```python
import torch

def _softmax(x, axis=-1):
    return torch.softmax(x, dim=axis)
def _gelu(x):
    return torch.nn.functional.gelu(x, approximate="tanh")
def _silu(x):
    return torch.nn.functional.silu(x)
def _sigmoid(x):
    return torch.sigmoid(x)
def _layernorm(x, axis=-1, eps=1e-5):
    m = x.mean(dim=axis, keepdim=True)
    v = x.var(dim=axis, keepdim=True, unbiased=False)
    return (x - m) / torch.sqrt(v + eps)
def _scatter_add(tgt, idx, src, axis=0):
    return tgt.clone().index_add_(axis, idx, src)
def _segment_sum(src, seg, n):
    out = torch.zeros((n,) + src.shape[1:], dtype=src.dtype)
    return out.index_add_(0, seg, src)
def _pad(x, pw):
    flat = tuple(v for pair in reversed(pw) for v in pair)
    return torch.nn.functional.pad(x, flat)

kernel_name = 'recurrent_scan__mha_block_cached__dense_activation_squared_relu__decode'

def setup_inputs():

    _w = (lambda a: (lambda t: t.to(torch.bfloat16) if t.is_floating_point() else t)(torch.as_tensor(a)))
    def _A_setup():
        B, Sq, D = 2, 1, 576
        H, Sk, Dh = 32, 4096, 128
        return {
            "x": _w((lambda *s: torch.randn(*s, dtype=torch.bfloat16) * 0.05)(B, Sq, D)),
            "k_cache": _w((lambda *s: torch.randn(*s, dtype=torch.bfloat16) * 0.8)(B, H, Sk, Dh)),
            "v_cache": _w((lambda *s: torch.randn(*s, dtype=torch.bfloat16) * 0.2)(B, H, Sk, Dh)),
            "wq": _w((lambda *s: torch.randn(*s, dtype=torch.bfloat16) * 0.8)(D, H * Dh) * (D ** -0.5)),
            "wo": _w((lambda *s: torch.randn(*s, dtype=torch.bfloat16) * 0.2)(H * Dh, D) * ((H * Dh) ** -0.5)),
        }
    _A_in = _A_setup()

    def _B_setup():
        B, S, D, F = 2, 1, 576, 4608
        return {
            "x": _w((lambda *s: torch.randn(*s, dtype=torch.bfloat16) * 0.05)(B, S, D)),
            "w1": _w((lambda *s: torch.randn(*s, dtype=torch.bfloat16) * 0.1)(D, F)),
            "b1": _w((lambda *s: torch.randn(*s, dtype=torch.bfloat16) * 0.8)(F)),
            "w2": _w((lambda *s: torch.randn(*s, dtype=torch.bfloat16) * 0.1)(F, D)),
            "b2": _w((lambda *s: torch.randn(*s, dtype=torch.bfloat16) * 12.8)(D)),
        }
    _B_in = _B_setup()

    return {
        "A_x": _A_in["x"],
        "A_k_cache": _A_in["k_cache"],
        "A_v_cache": _A_in["v_cache"],
        "A_wq": _A_in["wq"],
        "A_wo": _A_in["wo"],
        "B_x": _B_in["x"],
        "B_w1": _B_in["w1"],
        "B_b1": _B_in["b1"],
        "B_w2": _B_in["w2"],
        "B_b2": _B_in["b2"],
    }

def reference(A_x, A_k_cache, A_v_cache, A_wq, A_wo, B_x, B_w1, B_b1, B_w2, B_b2):
    def _A_ref(x, k_cache, v_cache, wq, wo):
        B, Sq, D = x.shape
        H, Dh = 32, 128
        q = (lambda x, *d: x.permute(*d))((x @ wq).reshape(B, Sq, H, Dh), 0, 2, 1, 3)
        scores = torch.einsum("bhqd,bhkd->bhqk", q, k_cache) * (Dh ** -0.5)
        p = _softmax(scores, axis=-1)
        o = torch.einsum("bhqk,bhkd->bhqd", p, v_cache)
        return (lambda x, *d: x.permute(*d))(o, 0, 2, 1, 3).reshape(B, Sq, H * Dh) @ wo

    def _B_ref(x, w1, b1, w2, b2):
        h = torch.einsum("bsd,df->bsf", x, w1) + b1
        zero = torch.zeros_like(h)
        r = torch.where(h > zero, h, zero)
        h = r * r
        out = torch.einsum("bsf,fd->bsd", h, w2) + b2
        return out

    x = A_x
    xn = _layernorm(x, axis=-1)
    s = _A_ref(xn, A_k_cache, A_v_cache, A_wq, A_wo)
    g = _silu(_B_ref(xn, B_w1, B_b1, B_w2, B_b2))
    return x + s * g
```

```cpp
#include <hip/hip_runtime.h>
#include <hip/hip_bf16.h>
#include <stdint.h>

typedef unsigned u32x4 __attribute__((ext_vector_type(4)));
typedef float f32x4 __attribute__((ext_vector_type(4)));
typedef __bf16 bf16x2 __attribute__((ext_vector_type(2)));

constexpr int B_ = 2, D_ = 576, H_ = 32, SK = 4096, DH = 128, F_ = 4608;
constexpr int NSPLIT = 4;

__device__ __forceinline__ float bf2f(unsigned short b) { return __builtin_bit_cast(float, (unsigned)b << 16); }
__device__ __forceinline__ float bflo(unsigned u) { return __builtin_bit_cast(float, u << 16); }
__device__ __forceinline__ float bfhi(unsigned u) { return __builtin_bit_cast(float, u & 0xffff0000u); }
__device__ __forceinline__ float rbf(float f) {
  return (float)(__bf16)f;
}
__device__ __forceinline__ unsigned short f2bf(float f) { return __builtin_bit_cast(unsigned short, (__bf16)f); }
__device__ __forceinline__ float dot2(unsigned k, unsigned q, float acc) {
  return __builtin_amdgcn_fdot2_f32_bf16(__builtin_bit_cast(bf16x2, k), __builtin_bit_cast(bf16x2, q), acc, false);
}

__device__ void layernorm(const unsigned short* x, float* xn, float* red) {
  const int tid = threadIdx.x;
  const int wave = tid >> 6, lane = tid & 63;
  if (wave < 2) {
    float v[9]; float s = 0.f;
    for (int i = 0; i < 9; ++i) { v[i] = bf2f(x[wave * D_ + i * 64 + lane]); s += v[i]; }
    for (int m = 32; m >= 1; m >>= 1) s += __shfl_xor(s, m);
    const float mean = s * (1.0f / D_);
    float ss = 0.f;
    for (int i = 0; i < 9; ++i) { const float d = v[i] - mean; ss += d * d; }
    for (int m = 32; m >= 1; m >>= 1) ss += __shfl_xor(ss, m);
    const float var = rbf(ss * (1.0f / D_));
    const float mb = rbf(mean);
    const float den = rbf(sqrtf(rbf(var + 1e-5f)));
    for (int i = 0; i < 9; ++i) xn[wave * D_ + i * 64 + lane] = rbf(rbf(v[i] - mb) / den);
  }
  __syncthreads();
}

__global__ void __launch_bounds__(256) k_pre(const unsigned short* x, const unsigned short* wq, const unsigned short* w1, const unsigned short* b1,
                                             unsigned short* q, float* h2) {
  __shared__ float xn[2 * D_];
  __shared__ float red[32 * 2 * 64];
  layernorm(x, xn, red);
  const int blk = blockIdx.x; const bool isq = blk < 64;
  const unsigned short* W = isq ? wq : w1; const int N = isq ? 4096 : F_; const int c0 = (isq ? blk : blk - 64) * 64;
  const int tid = threadIdx.x, cc = tid & 7, rg = tid >> 3;
  float a0[8] = {0}, a1[8] = {0};
  for (int r = rg; r < D_; r += 32) {
    const u32x4 w = *(const u32x4*)(W + (size_t)r * N + c0 + cc * 8);
    const float x0 = xn[r], x1 = xn[D_ + r];
    for (int k = 0; k < 4; ++k) { a0[2*k] += x0 * bflo(w[k]); a0[2*k+1] += x0 * bfhi(w[k]); a1[2*k] += x1 * bflo(w[k]); a1[2*k+1] += x1 * bfhi(w[k]); }
  }
  for (int k = 0; k < 8; ++k) { red[(rg * 2 + 0) * 64 + cc * 8 + k] = a0[k]; red[(rg * 2 + 1) * 64 + cc * 8 + k] = a1[k]; }
  __syncthreads();
  if (tid < 128) {
    const int b = tid >> 6, c = tid & 63; float s = 0.f;
    for (int g = 0; g < 32; ++g) s += red[(g * 2 + b) * 64 + c];
    if (isq) q[b * 4096 + c0 + c] = f2bf(s);
    else { float h = rbf(rbf(s) + bf2f(b1[c0 + c])); h = h > 0.f ? h : 0.f; h2[b * F_ + c0 + c] = rbf(h * h); }
  }
}

__global__ void __launch_bounds__(512, 2) k_attn(const unsigned short* q, const unsigned short* kc, const unsigned short* vc, float* part) {
  __shared__ float sm[8 * 132];
  const int blk = blockIdx.x; const int bh = blk >> 2, sp = blk & 3;
  const int tid = threadIdx.x, lane = tid & 63, wave = __builtin_amdgcn_readfirstlane(tid >> 6), r = lane >> 4, c = lane & 15;
  const u32x4 qv = *(const u32x4*)(q + bh * DH + c * 8);
  const size_t base = ((size_t)bh * SK + sp * 1024 + wave * 128) * DH;
  const u32x4* kp = (const u32x4*)(kc + base) + lane;
  const u32x4* vp = (const u32x4*)(vc + base) + lane;
  constexpr float C2 = 0.088388347648318440f * 1.4426950408889634f;
  u32x4 kr[32];
  for (int i = 0; i < 32; ++i) kr[i] = __builtin_nontemporal_load(kp + i * 64);
  __builtin_amdgcn_sched_barrier(0);
  float sc[32];
  for (int i = 0; i < 32; ++i) {
    float d = 0.f;
    for (int k = 0; k < 4; ++k) d = dot2(kr[i][k], qv[k], d);
    kr[i] = __builtin_nontemporal_load(vp + i * 64);
    d += __shfl_xor(d, 1); d += __shfl_xor(d, 2); d += __shfl_xor(d, 4); d += __shfl_xor(d, 8);
    sc[i] = d * C2;
    __builtin_amdgcn_sched_barrier(0);
  }
  float m = sc[0];
  for (int i = 1; i < 32; ++i) m = fmaxf(m, sc[i]);
  m = fmaxf(m, __shfl_xor(m, 16)); m = fmaxf(m, __shfl_xor(m, 32));
  float l = 0.f; float acc[8] = {0};
  for (int i = 0; i < 32; ++i) {
    const float p = __builtin_amdgcn_exp2f(sc[i] - m); l += p;
    for (int k = 0; k < 4; ++k) { acc[2*k] += p * bflo(kr[i][k]); acc[2*k+1] += p * bfhi(kr[i][k]); }
  }
  l += __shfl_xor(l, 16); l += __shfl_xor(l, 32);
  for (int k = 0; k < 8; ++k) { acc[k] += __shfl_xor(acc[k], 16); acc[k] += __shfl_xor(acc[k], 32); }
  if (r == 0) { for (int k = 0; k < 8; ++k) sm[wave * 132 + 4 + c * 8 + k] = acc[k]; if (c == 0) { sm[wave * 132] = m; sm[wave * 132 + 1] = l; } }
  __syncthreads();
  if (tid < 128) {
    float M = sm[0]; for (int w = 1; w < 8; ++w) M = fmaxf(M, sm[w * 132]);
    float L = 0.f, o = 0.f;
    for (int w = 0; w < 8; ++w) { const float e = __builtin_amdgcn_exp2f(sm[w * 132] - M); L += e * sm[w * 132 + 1]; o += e * sm[w * 132 + 4 + tid]; }
    part[blk * 132 + 4 + tid] = o;
    if (tid == 0) { part[blk * 132] = M; part[blk * 132 + 1] = L; }
  }
}

__global__ void __launch_bounds__(256) k_post(const float* part, const float* h2, const unsigned short* wo, const unsigned short* w2, float* ys) {
  __shared__ float v[2 * 64];
  __shared__ float red[3 * 2 * 576];
  const int blk = blockIdx.x; const bool isa = blk < 64; const int r0 = (isa ? blk : blk - 64) * 64;
  const int tid = threadIdx.x;
  if (tid < 128) {
    const int b = tid >> 6, j = tid & 63;
    if (isa) {
      const int h = (r0 + j) >> 7, d = (r0 + j) & 127; const float* p = part + (size_t)((b * 32 + h) * 4) * 132;
      float M = p[0]; for (int s = 1; s < 4; ++s) M = fmaxf(M, p[s * 132]);
      float L = 0.f, o = 0.f;
      for (int s = 0; s < 4; ++s) { const float e = __builtin_amdgcn_exp2f(p[s * 132] - M); L += e * p[s * 132 + 1]; o += e * p[s * 132 + 4 + d]; }
      v[tid] = rbf(o / L);
    } else v[tid] = h2[b * F_ + r0 + j];
  }
  __syncthreads();
  const unsigned short* W = isa ? wo : w2;
  if (tid < 216) {
    const int cc = tid % 72, rg = tid / 72;
    float a0[8] = {0}, a1[8] = {0};
    for (int r = rg; r < 64; r += 3) {
      const u32x4 w = *(const u32x4*)(W + (size_t)(r0 + r) * D_ + cc * 8);
      const float x0 = v[r], x1 = v[64 + r];
      for (int k = 0; k < 4; ++k) { a0[2*k] += x0 * bflo(w[k]); a0[2*k+1] += x0 * bfhi(w[k]); a1[2*k] += x1 * bflo(w[k]); a1[2*k+1] += x1 * bfhi(w[k]); }
    }
    for (int k = 0; k < 8; ++k) { red[(rg * 2 + 0) * 576 + cc * 8 + k] = a0[k]; red[(rg * 2 + 1) * 576 + cc * 8 + k] = a1[k]; }
  }
  __syncthreads();
  for (int i = tid; i < 2 * 576; i += 256) ys[(size_t)blk * 1152 + i] = red[i] + red[1152 + i] + red[2304 + i];
}

__global__ void __launch_bounds__(256) k_fin(const float* ys, const unsigned short* x, const unsigned short* b2, unsigned short* out) {
  const int i = blockIdx.x * 64 + (threadIdx.x & 63); const int g = threadIdx.x >> 6;
  __shared__ float rs[4][64], rg[4][64];
  float s = 0.f, y = 0.f;
  for (int k = g; k < 64; k += 4) s += ys[(size_t)k * 1152 + i];
  for (int k = g; k < 72; k += 4) y += ys[(size_t)(64 + k) * 1152 + i];
  rs[g][threadIdx.x & 63] = s; rg[g][threadIdx.x & 63] = y;
  __syncthreads();
  if (g == 0) {
    const int t = threadIdx.x;
    s = rbf(rs[0][t] + rs[1][t] + rs[2][t] + rs[3][t]);
    y = rbf(rbf(rg[0][t] + rg[1][t] + rg[2][t] + rg[3][t]) + bf2f(b2[i % 576]));
    const float gg = rbf(y / (1.0f + __expf(-y)));
    out[i] = f2bf(bf2f(x[i]) + rbf(s * gg));
  }
}

extern "C" void kernel_launch(void* const* d_in, const int* in_sizes, int n_in, void* d_out, int out_size, void* d_ws, size_t ws_size, hipStream_t stream) {
  const unsigned short* x = (const unsigned short*)d_in[0];
  const unsigned short* kc = (const unsigned short*)d_in[1];
  const unsigned short* vc = (const unsigned short*)d_in[2];
  const unsigned short* wq = (const unsigned short*)d_in[3];
  const unsigned short* wo = (const unsigned short*)d_in[4];
  const unsigned short* w1 = (const unsigned short*)d_in[6];
  const unsigned short* b1 = (const unsigned short*)d_in[7];
  const unsigned short* w2 = (const unsigned short*)d_in[8];
  const unsigned short* b2 = (const unsigned short*)d_in[9];
  char* ws = (char*)d_ws;
  unsigned short* q = (unsigned short*)ws;
  float* h2 = (float*)(ws + 65536);
  float* part = (float*)(ws + 131072);
  float* ys = (float*)(ws + 524288);
  k_pre<<<136, 256, 0, stream>>>(x, wq, w1, b1, q, h2);
  k_attn<<<256, 512, 0, stream>>>(q, kc, vc, part);
  k_post<<<136, 256, 0, stream>>>(part, h2, wo, w2, ys);
  k_fin<<<18, 256, 0, stream>>>(ys, x, b2, (unsigned short*)d_out);
}
```

```cpp
#include <hip/hip_runtime.h>
#include <stdint.h>
#include <utility>

typedef unsigned u32;
typedef unsigned long long u64;
typedef unsigned u32x4 __attribute__((ext_vector_type(4)));
typedef unsigned u32x2 __attribute__((ext_vector_type(2)));
typedef float f32x4 __attribute__((ext_vector_type(4)));
typedef float f32x2 __attribute__((ext_vector_type(2)));
typedef __bf16 bf16x8 __attribute__((ext_vector_type(8)));
typedef __bf16 bf16x2 __attribute__((ext_vector_type(2)));
#define LAS __attribute__((address_space(3)))
#define GAS __attribute__((address_space(1)))
typedef __amdgpu_buffer_rsrc_t rsrc_t;

template <class F, int... I> __device__ __forceinline__ void sfor_impl(F&& f, std::integer_sequence<int, I...>) { (f(std::integral_constant<int, I>{}), ...); }
template <int N, class F> __device__ __forceinline__ void sfor(F&& f) { sfor_impl(f, std::make_integer_sequence<int, N>{}); }
template <int V> using ic = std::integral_constant<int, V>;

constexpr int DM = 576, SK = 4096, DH = 128, FF = 4608;
constexpr u32 WS_E  = 0;
constexpr u32 WS_QP = 4096;
constexpr u32 WS_HP = WS_QP + 32 * 8 * 2048;
constexpr u32 WS_OX = WS_HP + 32 * 8 * 2304;
constexpr u32 WS_OS = WS_OX + 32 * 8 * 1024;
constexpr u32 WS_YA = WS_OS + 32 * 8 * 128;
constexpr u32 YAS = 256 * 64 + 256, YFS = 64 * 64 + 256;
constexpr u32 WS_YF = WS_YA + 144 * YAS;
constexpr u32 WS_END = WS_YF + 144 * YFS;
constexpr u32 L_PQ = 0;
constexpr u32 L_PH = L_PQ + 4096;
constexpr u32 L_OP = L_PH + 4608;
constexpr u32 L_ML = L_OP + 2048;
constexpr u32 L_QP = L_ML + 32;
constexpr u32 L_FIN = L_QP + 2048;
constexpr u32 YW = 1280;
constexpr u32 L_Y = L_FIN + 256;
constexpr u32 L_A = L_Y + 4 * YW;
constexpr u32 L_TAG = L_A + 1024;
constexpr u32 L_END = 96 * 1024;
static_assert(L_TAG + 16 <= L_END && L_A % 16 == 0 && L_Y % 16 == 0 && L_OP % 16 == 0, "lds");

constexpr int RING = 32;
constexpr int IV = 64, IW2 = 128, IWO = 138, NITEM = 146;
constexpr int THR = 10;
constexpr int WIN = 14;
constexpr int NINIT0 = 8;
constexpr int NINIT = 14;
constexpr int QPOS = 15;
constexpr int HP1 = 18;
constexpr int QCHK = 23;
constexpr int SYNC = 16;
constexpr int PT0 = 200, PD = 216;
constexpr int PSL0 = 14, PSL1 = 6, PSL2 = 6;
static_assert(NINIT0 <= NINIT && NINIT <= QPOS && QPOS <= HP1 && HP1 <= QCHK && NINIT <= RING - 15 && HP1 <= RING - 10 && QCHK <= RING, "");
constexpr u32 OOB = 0x40000000u;
constexpr int AUX_NT = 2, AUX_SC1 = 16;
constexpr u32 SPIN_CAP = 1u << 15;

__device__ __forceinline__ float bflo(u32 u) { return __builtin_bit_cast(float, u << 16); }
__device__ __forceinline__ float bfhi(u32 u) { return __builtin_bit_cast(float, u & 0xffff0000u); }
__device__ __forceinline__ float rbf(float f) { return (float)(__bf16)f; }
__device__ __forceinline__ u32 pkbf(float lo, float hi) { bf16x2 v = {(__bf16)lo, (__bf16)hi}; return __builtin_bit_cast(u32, v); }
__device__ __forceinline__ float dot2(u32 a, u32 b, float acc) {
  return __builtin_amdgcn_fdot2_f32_bf16(__builtin_bit_cast(bf16x2, a), __builtin_bit_cast(bf16x2, b), acc, false);
}
template <int CTRL> __device__ __forceinline__ float dpp(float x) {
  return __builtin_bit_cast(float, __builtin_amdgcn_mov_dpp(__builtin_bit_cast(int, x), CTRL, 0xf, 0xf, true));
}
template <int CTRL> __device__ __forceinline__ u32 dppu(u32 x) { return (u32)__builtin_amdgcn_mov_dpp((int)x, CTRL, 0xf, 0xf, true); }
constexpr int XOR1 = 0xB1, XOR2 = 0x4E, HMIR = 0x141, ROR1 = 0x121, ROR2 = 0x122, ROR4 = 0x124, ROR8 = 0x128, BCAST = 0x150;
__device__ __forceinline__ float x16_sum(float x) {
  auto s = __builtin_amdgcn_permlane16_swap(__float_as_uint(x), __float_as_uint(x), false, false);
  return __uint_as_float(s[0]) + __uint_as_float(s[1]);
}
__device__ __forceinline__ float x32_sum(float x) {
  auto s = __builtin_amdgcn_permlane32_swap(__float_as_uint(x), __float_as_uint(x), false, false);
  return __uint_as_float(s[0]) + __uint_as_float(s[1]);
}
__device__ __forceinline__ float x16_max(float x) {
  auto s = __builtin_amdgcn_permlane16_swap(__float_as_uint(x), __float_as_uint(x), false, false);
  return fmaxf(__uint_as_float(s[0]), __uint_as_float(s[1]));
}
__device__ __forceinline__ float x32_max(float x) {
  auto s = __builtin_amdgcn_permlane32_swap(__float_as_uint(x), __float_as_uint(x), false, false);
  return fmaxf(__uint_as_float(s[0]), __uint_as_float(s[1]));
}
__device__ __forceinline__ float row_sum(float x) { x += dpp<ROR8>(x); x += dpp<ROR4>(x); x += dpp<ROR2>(x); x += dpp<ROR1>(x); return x; }
__device__ __forceinline__ float row_max(float x) { x = fmaxf(x, dpp<ROR8>(x)); x = fmaxf(x, dpp<ROR4>(x)); x = fmaxf(x, dpp<ROR2>(x)); x = fmaxf(x, dpp<ROR1>(x)); return x; }
__device__ __forceinline__ f32x4 mfma(u32x4 a, u32x4 b, f32x4 c) {
  return __builtin_amdgcn_mfma_f32_16x16x32_bf16(__builtin_bit_cast(bf16x8, a), __builtin_bit_cast(bf16x8, b), c, 0, 0, 0);
}
constexpr int bitrev4(int i) { return ((i & 1) << 3) | ((i & 2) << 1) | ((i & 4) >> 1) | ((i & 8) >> 3); }
#define SB() __builtin_amdgcn_sched_barrier(0)
#define POLL() asm volatile("; poll" ::: "memory")
#define VM0() __builtin_amdgcn_s_waitcnt(0x0F70)
#define LGKM0() asm volatile("s_waitcnt lgkmcnt(0)" ::: "memory")
#define INFLIGHT(n) asm volatile("s_waitcnt vmcnt(%0)" :: "n"((n) - 1) : "memory")
__device__ __forceinline__ void wg_barrier() { LGKM0(); __builtin_amdgcn_s_barrier(); asm volatile("" ::: "memory"); }

#define GSTORE(v, r, o) __builtin_amdgcn_raw_buffer_store_b128(v, r, o, 0, AUX_SC1)

struct Tag { u32 lo, hi; };
__device__ __forceinline__ u32 genc(float v, u32 tlo) { return ((__float_as_uint(v) + 0x80u) & 0xFFFFFF00u) | tlo; }
__device__ __forceinline__ float gdec(u32 d) { return __uint_as_float(d & 0xFFFFFF00u); }
__device__ __forceinline__ u32 gbad(u32 d0, u32 d1, Tag t) { return ((d0 ^ t.lo) & 0xFFu) | (d1 ^ t.hi); }
__device__ __forceinline__ u32 gbad4(u32x4 d, Tag t) { return gbad(d[0], d[1], t) | gbad(d[2], d[3], t); }
__device__ __forceinline__ u32x4 gpair(float a, float b, Tag t) { return u32x4{genc(a, t.lo), t.hi, genc(b, t.lo), t.hi}; }
__device__ __forceinline__ bool wave_any(u32 bad) { return __builtin_amdgcn_ballot_w64(bad != 0) != 0; }

__global__ void __launch_bounds__(256, 1)
fused_decode(const unsigned short* x, const char* kc, const char* vc, const char* wq, const char* wo, const char* w1, const unsigned short* b1,
             const char* w2, const unsigned short* b2, unsigned short* out, char* ws) {
  asm volatile("" : "+s"(x), "+s"(kc), "+s"(vc), "+s"(wq), "+s"(wo), "+s"(w1), "+s"(b1), "+s"(w2), "+s"(b2), "+s"(out), "+s"(ws));
  extern __shared__ __attribute__((aligned(16))) char smem_[];
  LAS char* const L = (LAS char*)smem_;
  const int tid = threadIdx.x, lane = tid & 63, wave = __builtin_amdgcn_readfirstlane(tid >> 6);
  const int g = lane >> 4, c = lane & 15;
  const int bid = blockIdx.x;
  const int xl = bid & 7, ii = bid >> 3;
  const int h = xl * 4 + (ii >> 3), j8 = ii & 7, b = j8 >> 2, sp = j8 & 3;
  const u32 tp0 = (u32)__builtin_amdgcn_s_memrealtime() + PT0;
  auto pace = [&](int n) { const u32 tgt = tp0 + (u32)(n * PD / 16); while ((int)((u32)__builtin_amdgcn_s_memrealtime() - tgt) < 0) __builtin_amdgcn_s_sleep(1); };

  const rsrc_t rsW = __builtin_amdgcn_make_buffer_rsrc((void*)ws, 0, WS_END, 0x00020000);
  const size_t kvbase = ((size_t)(b * 32 + h) * SK + sp * 1024) * (DH * 2);
  const rsrc_t rsK = __builtin_amdgcn_make_buffer_rsrc((void*)(kc + kvbase), 0, 262144, 0x00020000);
  const rsrc_t rsV = __builtin_amdgcn_make_buffer_rsrc((void*)(vc + kvbase), 0, 262144, 0x00020000);
  const u32 voKV = wave * 4096 + lane * 16;

  u32x4 ring[RING];
  auto issue = [&](auto nn) {
    constexpr int n = decltype(nn)::value; constexpr int s = n % RING;
    if constexpr (n < IV) ring[s] = __builtin_amdgcn_raw_buffer_load_b128(rsK, voKV + (n & 3) * 1024, (n >> 2) * 16384, AUX_NT);
    else if constexpr (n < IW2) { constexpr int k = n - IV; ring[s] = __builtin_amdgcn_raw_buffer_load_b128(rsV, voKV + (k & 3) * 1024, (k >> 2) * 16384, AUX_NT); }
    SB();
  };
  const int lh = lane & 31, bb = lane >> 5;
  const int m8 = c & 7;
  u32 msk[4];
#pragma unroll
  for (int d = 0; d < 4; ++d) msk[d] = (m8 >> 1) == d ? ((m8 & 1) ? 0xFFFF0000u : 0x0000FFFFu) : 0u;
  auto amask = [&](u32 v) { return u32x4{v & msk[0], v & msk[1], v & msk[2], v & msk[3]}; };
  const int t0w = wave * 4 + (wave < 2 ? wave : 2);
  const int fa = b, fc = sp;
  const int rs_ = (ii >> 2) * 8 + xl, rq = ii & 3;
  const bool reducer = rs_ < 36;
  const int rb = reducer && rs_ >= 18, rs1 = reducer ? rs_ - 18 * rb : 0, rw = rs1 >> 2, ra = rs1 & 3, rhalf = rq >> 1, rrr = rq & 1;
  const int rid = 72 * rb + (rw < 4 ? ((rw * 2 + rhalf) * 4 + ra) * 2 + rrr : 64 + (rhalf * 2 + ra) * 2 + rrr);
  const int ocol = 128 * rw + 32 * ra + 4 * rhalf + 2 * rrr + 8 * ((lane & 7) >> 1) + (lane & 1);

  const rsrc_t rsX = __builtin_amdgcn_make_buffer_rsrc((void*)x, 0, 2 * DM * 2, 0x00020000);
  u32x4 xs_[3]; u32 xe[5];
#pragma unroll
  for (int i = 0; i < 3; ++i) xs_[i] = __builtin_amdgcn_raw_buffer_load_b128(rsX, bb * (DM * 2) + (lh < 24 ? lh : 0) * 16, i * 384, 0);
#pragma unroll
  for (int t = 0; t < 5; ++t) xe[t] = __builtin_amdgcn_raw_buffer_load_b16(rsX, ((c >> 3) * DM + 72 * j8 + 4 * t0w + g) * 2 + (t < 4 || wave < 2 ? t * 8 : 0), 0, 0);
  SB();
  const rsrc_t rsQ = __builtin_amdgcn_make_buffer_rsrc((void*)wq, 0, DM * 8192, 0x00020000);
  const u32 voQ = (u32)(72 * j8 + 4 * t0w + g) * 8192 + h * 256 + c * 16, voQl = wave < 2 ? voQ : OOB;
  sfor<5>([&](auto tt) { constexpr int t = decltype(tt)::value; ring[RING - 15 + t] = __builtin_amdgcn_raw_buffer_load_b128(rsQ, t < 4 ? voQ : voQl, t * 4 * 8192, AUX_NT); SB(); });
  u32x2 ep;
  asm volatile("" : "=v"(ep));
  if (wave == 0) ep = __builtin_amdgcn_raw_buffer_load_b64(rsW, WS_E, 0, AUX_SC1);
  SB();
  __builtin_amdgcn_s_barrier();
  sfor<NINIT0>([&](auto kk) { issue(kk); });

  u32 xsA[5];
  {
    float v[24];
#pragma unroll
    for (int i = 0; i < 3; ++i)
#pragma unroll
      for (int k = 0; k < 4; ++k) { v[i * 8 + 2 * k] = bflo(xs_[i][k]); v[i * 8 + 2 * k + 1] = bfhi(xs_[i][k]); }
    float s4[4] = {0.f, 0.f, 0.f, 0.f};
#pragma unroll
    for (int i = 0; i < 24; ++i) s4[i & 3] += v[i];
    float s = (s4[0] + s4[1]) + (s4[2] + s4[3]);
    s = lh < 24 ? s : 0.f;
    s = x16_sum(row_sum(s));
    const float mean = s * (1.0f / DM);
    float q4[4] = {0.f, 0.f, 0.f, 0.f};
#pragma unroll
    for (int i = 0; i < 24; ++i) { const float d = v[i] - mean; q4[i & 3] = fmaf(d, d, q4[i & 3]); }
    float ss = (q4[0] + q4[1]) + (q4[2] + q4[3]);
    ss = lh < 24 ? ss : 0.f;
    ss = x16_sum(row_sum(ss));
    const float mb = rbf(mean);
    const float rden = __builtin_amdgcn_rcpf(rbf(__builtin_amdgcn_sqrtf(rbf(rbf(ss * (1.0f / DM)) + 1e-5f))));
    const auto mm = __builtin_amdgcn_permlane32_swap(__float_as_uint(mb), __float_as_uint(mb), false, false);
    const auto rr = __builtin_amdgcn_permlane32_swap(__float_as_uint(rden), __float_as_uint(rden), false, false);
    const float ms = __uint_as_float((c & 8) ? mm[1] : mm[0]), rs = __uint_as_float((c & 8) ? rr[1] : rr[0]);
#pragma unroll
    for (int t = 0; t < 5; ++t) { const float a = rbf(bflo(xe[t]) - ms) * rs; xsA[t] = (t == 4 && wave >= 2) ? 0u : pkbf(a, a); }
  }
  SB();

  sfor<NINIT - NINIT0>([&](auto kk) { issue(ic<NINIT0 + decltype(kk)::value>{}); });
  Tag tag;
  {
    f32x4 D0 = {0.f, 0.f, 0.f, 0.f}, D1 = {0.f, 0.f, 0.f, 0.f};
#pragma unroll
    for (int t = 0; t < 5; ++t) { if (t & 1) D1 = mfma(amask(xsA[t]), ring[RING - 15 + t], D1); else D0 = mfma(amask(xsA[t]), ring[RING - 15 + t], D0); }
    *(LAS f32x4*)(L + L_PQ + wave * 1024 + (bb * 128 + 8 * c + 4 * (g & 1)) * 4) = D0 + D1;
    asm volatile("" : "+v"(ep));
    if (wave == 0 && lane == 0) { const u64 e1_ = (((u64)ep[1] << 32) | ep[0]) + 1; *(LAS u32x2*)(L + L_TAG) = u32x2{(u32)e1_ & 0xFFu, (u32)(e1_ >> 8)}; }

    wg_barrier();

    { const u32x2 t = *(const LAS u32x2*)(L + L_TAG); tag.lo = __builtin_amdgcn_readfirstlane(t[0]); tag.hi = __builtin_amdgcn_readfirstlane(t[1]); }
    f32x2 s = {0.f, 0.f};
#pragma unroll
    for (int w = 0; w < 4; ++w) s += *(const LAS f32x2*)(L + L_PQ + w * 1024 + (wave * 32 + lh) * 8);
    if (lane < 32) GSTORE(gpair(s[0], s[1], tag), rsW, WS_QP + (h * 8 + j8) * 2048 + (wave * 32 + lh) * 16);
    SB();
  }

  const rsrc_t rsW1 = __builtin_amdgcn_make_buffer_rsrc((void*)w1, 0, DM * 9216, 0x00020000);
  const u32 vo1 = (u32)(72 * j8 + 4 * t0w + g) * 9216 + h * 288;
  const u32 vo1a = vo1 + c * 16, vo1b = vo1 + 256 + (c & 1) * 16, vo1al = wave < 2 ? vo1a : OOB, vo1bl = wave < 2 ? vo1b : OOB;
  sfor<10>([&](auto tt) { constexpr int n = decltype(tt)::value, t = n >> 1;
    if constexpr ((n & 1) == 0) asm volatile("s_waitcnt vmcnt(%0)" :: "n"(THR - 1 + (n >> 1)) : "memory");
    ring[RING - 10 + n] = __builtin_amdgcn_raw_buffer_load_b128(rsW1, t < 4 ? ((n & 1) ? vo1b : vo1a) : ((n & 1) ? vo1bl : vo1al), t * 4 * 9216, AUX_NT); SB(); });
  sfor<QPOS - NINIT>([&](auto kk) { asm volatile("s_waitcnt vmcnt(%0)" :: "n"(THR + 4 - (int)decltype(kk)::value > THR - 1 ? THR + 4 - (int)decltype(kk)::value : THR - 1) : "memory"); issue(ic<NINIT + decltype(kk)::value>{}); });
  u32x4 qg[2];
  const u32 qoff = WS_QP + (h * 8 + 2 * wave) * 2048 + b * 1024 + lane * 16;
  auto qload = [&]() { qg[0] = __builtin_amdgcn_raw_buffer_load_b128(rsW, qoff, 0, AUX_SC1); qg[1] = __builtin_amdgcn_raw_buffer_load_b128(rsW, qoff, 2048, AUX_SC1); SB(); };

  qload();
  sfor<HP1 - QPOS>([&](auto kk) { INFLIGHT(THR); issue(ic<QPOS + decltype(kk)::value>{}); });
  {
    f32x4 Da = {0.f, 0.f, 0.f, 0.f}, Db = {0.f, 0.f, 0.f, 0.f};
#pragma unroll
    for (int t = 0; t < 5; ++t) { const u32x4 A = amask(xsA[t]); Da = mfma(A, ring[RING - 10 + 2 * t], Da); Db = mfma(A, ring[RING - 9 + 2 * t], Db); }
    LAS char* pb = L + L_PH + wave * 1152 + (bb * 144 + 8 * c + 4 * (g & 1)) * 4;
    *(LAS f32x4*)pb = Da;
    if (c < 2) *(LAS f32x4*)(pb + 512) = Db;

    wg_barrier();

    const int tl = lane < 36 ? lane : 0, tk = wave * 36 + tl;
    f32x2 s = {0.f, 0.f};
#pragma unroll
    for (int w = 0; w < 4; ++w) s += *(const LAS f32x2*)(L + L_PH + w * 1152 + ((wave & 1) * 144 + 72 * (wave >> 1) + 2 * tl) * 4);
    if (lane < 36) GSTORE(gpair(s[0], s[1], tag), rsW, WS_HP + (h * 8 + j8) * 2304 + tk * 16);
    SB();
  }
  sfor<QCHK - HP1>([&](auto kk) { INFLIGHT(THR); issue(ic<HP1 + decltype(kk)::value>{}); });
  {
    u32 spin = 0;
    for (; wave_any(gbad4(qg[0], tag) | gbad4(qg[1], tag)) && spin < SPIN_CAP; ++spin) { POLL(); qload(); VM0(); }
    *(LAS f32x2*)(L + L_QP + wave * 512 + lane * 8) = f32x2{gdec(qg[0][0]) + gdec(qg[1][0]), gdec(qg[0][2]) + gdec(qg[1][2])};
    sfor<RING - QCHK>([&](auto kk) { INFLIGHT(THR); issue(ic<QCHK + decltype(kk)::value>{}); });
  }
  wg_barrier();
  u32x4 qv;
  {
    f32x4 qa = {0.f, 0.f, 0.f, 0.f}, qb = {0.f, 0.f, 0.f, 0.f};
#pragma unroll
    for (int w = 0; w < 4; ++w) { const LAS f32x4* p = (const LAS f32x4*)(L + L_QP + w * 512 + c * 32); qa += p[0]; qb += p[1]; }
    qv = u32x4{pkbf(qa[0], qa[1]), pkbf(qa[2], qa[3]), pkbf(qb[0], qb[1]), pkbf(qb[2], qb[3])};
  }

  const bool cb8 = c & 8, cb4 = c & 4, cb2 = c & 2, cb1 = c & 1;
  float sc[4]; float e1[2], f1[2], g1[2];
  sfor<64>([&](auto kk) { constexpr int k = decltype(kk)::value; constexpr int i = k & 15;
    const u32x4 kv = ring[k % RING];
    float d = dot2(kv[0], qv[0], 0.f); d = dot2(kv[1], qv[1], d); d = dot2(kv[2], qv[2], d); d = dot2(kv[3], qv[3], d);
    SB();
    if constexpr (k % SYNC == 0 && k > 0) __builtin_amdgcn_s_barrier();
    if constexpr ((k + RING) % 2 == 0) pace(k + RING);
    INFLIGHT(WIN);
    issue(ic<k + RING>{});
    d += dpp<ROR8>(d);
    if constexpr ((i & 1) == 0) e1[0] = d;
    else {
      float e = cb8 ? d : e1[0]; e += dpp<HMIR>(e);
      if constexpr ((i & 2) == 0) f1[0] = e;
      else {
        float f = cb4 ? e : f1[0]; f += dpp<XOR2>(f);
        if constexpr ((i & 4) == 0) g1[0] = f;
        else {
          float q = cb2 ? f : g1[0]; q += dpp<XOR1>(q);
          if constexpr ((i & 8) == 0) g1[1] = q;
          else sc[k >> 4] = cb1 ? q : g1[1];
        }
      }
    }
    SB();
  });

  constexpr float C2 = 0.088388347648318440f * 1.4426950408889634f;
  float mw = fmaxf(fmaxf(sc[0], sc[1]), fmaxf(sc[2], sc[3]));
  mw = x32_max(x16_max(row_max(mw))) * C2;
  float lw = 0.f; u32 pd[4];
#pragma unroll
  for (int t = 0; t < 4; ++t) { const float p = __builtin_amdgcn_exp2f(fmaf(sc[t], C2, -mw)); lw += p; pd[t] = pkbf(p, p); }
  lw = x32_sum(x16_sum(row_sum(lw)));
  if (lane == 0) *(LAS f32x2*)(L + L_ML + wave * 8) = f32x2{mw, lw};
  SB();

  const rsrc_t rsW2 = __builtin_amdgcn_make_buffer_rsrc((void*)w2, 0, FF * 1152, 0x00020000);
  const int fcol = 288 * (fc >> 1) + 160 * (fc & 1);
  const u32 vo2 = (u32)(144 * h + 72 * fa + 4 * t0w + g) * 1152 + fcol * 2;
  const u32 vo2a = vo2 + c * 16, vo2b = (fc & 1) ? OOB : vo2 + 256 + (c & 3) * 16;
  const u32 vo2al = wave < 2 ? vo2a : OOB, vo2bl = wave < 2 ? vo2b : OOB;
  const rsrc_t rsO = __builtin_amdgcn_make_buffer_rsrc((void*)wo, 0, 4096 * 1152, 0x00020000);
  const u32 voO = (u32)(128 * h + 16 * j8 + g) * 1152;
  const u32 voOa = voO + wave * 256 + c * 16, voOb = wave == 3 ? voO + 1024 + (c & 7) * 16 : OOB;
  auto issue2 = [&](auto nn) { constexpr int n = decltype(nn)::value;
    if constexpr (n < IW2) issue(nn);
    else if constexpr (n < IWO) { constexpr int k = n - IW2, tt = k >> 1;
      ring[n % RING] = __builtin_amdgcn_raw_buffer_load_b128(rsW2, tt < 4 ? ((k & 1) ? vo2b : vo2a) : ((k & 1) ? vo2bl : vo2al), tt * 4608, AUX_NT); SB(); }
    else if constexpr (n < NITEM) { constexpr int k = n - IWO;
      ring[n % RING] = __builtin_amdgcn_raw_buffer_load_b128(rsO, k < 4 ? voOa : voOb, (k & 3) * 4608, AUX_NT); SB(); }
  };
  u32x4 hg[3]; u32 b1v[5]; u32 xsF[5];
  u32 hof[3];
#pragma unroll
  for (int j = 0; j < 3; ++j) { const int i = 64 * j + lane; hof[j] = i < 144 ? WS_HP + (h * 8 + 2 * wave) * 2304 + fa * 1152 + (i < 72 ? i * 16 : 2304 + (i - 72) * 16) : OOB; }
  auto hload = [&]() {
#pragma unroll
    for (int j = 0; j < 3; ++j) hg[j] = __builtin_amdgcn_raw_buffer_load_b128(rsW, hof[j], 0, AUX_SC1); };
  auto hbad = [&]() { return wave_any(gbad4(hg[0], tag) | gbad4(hg[1], tag) | (lane < 16 ? gbad4(hg[2], tag) : 0u)); };
  u32 xraw, braw;
  f32x4 O = {0.f, 0.f, 0.f, 0.f};
  auto small_loads = [&]() {
    hload();
#pragma unroll
    for (int t = 0; t < 5; ++t) { const int tq = (t == 4 && wave >= 2) ? 3 : t; b1v[t] = ((const GAS unsigned short*)b1)[144 * h + 72 * fa + 4 * (t0w + tq) + g]; }
    xraw = ((const GAS unsigned short*)x)[rb * DM + ocol]; braw = ((const GAS unsigned short*)b2)[ocol];
    SB();
  };
  auto h_to_lds = [&]() {
    u32 spin = 0;
    for (; hbad() && spin < SPIN_CAP; ++spin) { POLL(); hload(); VM0(); }
#pragma unroll
    for (int j = 0; j < 3; ++j) { const int i = 64 * j + lane;
      if (j < 2 || lane < 16) *(LAS f32x2*)(L + L_PH + (2 * wave) * 576 + i * 8) = f32x2{gdec(hg[j][0]), gdec(hg[j][2])}; }
    SB();
  };
  auto h_finish = [&]() {
#pragma unroll
    for (int t = 0; t < 5; ++t) {
      const int tq = (t == 4 && wave >= 2) ? 3 : t;
      float v = *(const LAS float*)(L + L_PH + (c & 7) * 576 + ((c >> 3) * 72 + 4 * (t0w + tq) + g) * 4);
      v += dpp<XOR1>(v); v += dpp<XOR2>(v); v += dpp<HMIR>(v);
      float hh = rbf(rbf(v) + bflo(b1v[t])); hh = hh > 0.f ? hh : 0.f;
      const float h2 = hh * hh;
      xsF[t] = (t == 4 && wave >= 2) ? 0u : pkbf(h2, h2);
      asm volatile("" : "+v"(xsF[t]));
    }
    SB();
  };
  constexpr int VL = 64 - RING;
  constexpr int VT = 64 - WIN;
  sfor<64>([&](auto kk) { constexpr int k = decltype(kk)::value; constexpr int n = IV + k, i = k & 15;
    const u32 pb = dppu<BCAST + bitrev4(i)>(pd[k >> 4]);
    O = mfma(amask(pb), ring[n % RING], O);
    asm volatile("" : "+a"(O));
    SB();
    if constexpr (k == 15) h_to_lds();
    if constexpr (k == 16) wg_barrier();
    if constexpr (k == 17) h_finish();
    if constexpr (k < VL) {
      if constexpr (k % SYNC == 0 && k != 16) __builtin_amdgcn_s_barrier();
      if constexpr ((n + RING) % 2 == 0) pace(n + RING);
      asm volatile("s_waitcnt vmcnt(%0)" :: "n"(k >= 2 && k <= WIN ? WIN + 9 : WIN - 1) : "memory");
      issue2(ic<n + RING>{});
      if constexpr (k == 1) small_loads();
    } else if constexpr (k >= VT - 3) {
      constexpr int j = k - (VT - 3);
      if constexpr (j < 5) { issue2(ic<IW2 + 2 * j>{}); issue2(ic<IW2 + 2 * j + 1>{}); }
      else if constexpr (j < 9) { issue2(ic<IWO + j - 5>{}); issue2(ic<IWO + 4 + j - 5>{}); }
    }
  });
  static_assert(VT - 3 + 9 <= 64 && VL > 17 && SYNC == 16 && RING >= WIN + 12, "a trailing tile must not land in the slot of a V tile that is still needed");

  if (lane < 32) *(LAS f32x4*)(L + L_OP + wave * 512 + (8 * c + 4 * g) * 4) = O;
  wg_barrier();
  const u32 opr = h * 8 + (c >> 3) * 4 + (c & 3);
  const u32 ooff = WS_OX + opr * 1024 + j8 * 128 + ((c >> 2) & 1) * 64 + g * 16, osoff = WS_OS + opr * 128;
  if (wave == 0) {
    const f32x4 ml0 = *(const LAS f32x4*)(L + L_ML), ml1 = *(const LAS f32x4*)(L + L_ML + 16);
    const LAS float* op = (const LAS float*)(L + L_OP) + 16 * (lane >> 3) + 8 * ((lane >> 2) & 1) + (lane & 3);
    float oa[4], ob[4];
#pragma unroll
    for (int w = 0; w < 4; ++w) { oa[w] = op[w * 128]; ob[w] = op[w * 128 + 4]; }
    const float M = gdec(genc(fmaxf(fmaxf(ml0[0], ml0[2]), fmaxf(ml1[0], ml1[2])), 0));
    const float w0 = __builtin_amdgcn_exp2f(ml0[0] - M), w1_ = __builtin_amdgcn_exp2f(ml0[2] - M), w2_ = __builtin_amdgcn_exp2f(ml1[0] - M), w3 = __builtin_amdgcn_exp2f(ml1[2] - M);
    const float pv0 = (oa[0] * w0 + oa[1] * w1_) + (oa[2] * w2_ + oa[3] * w3), pv1 = (ob[0] * w0 + ob[1] * w1_) + (ob[2] * w2_ + ob[3] * w3);
    GSTORE(gpair(pv0, pv1, tag), rsW, WS_OX + (h * 8 + j8) * 1024 + lane * 16);
    const float Ls = (ml0[1] * w0 + ml0[3] * w1_) + (ml1[1] * w2_ + ml1[3] * w3);
    if (lane < 4) GSTORE(gpair(M, Ls, tag), rsW, WS_OS + (h * 8 + j8) * 128 + lane * 16);
  }
  SB();

  {
    f32x4 accFa = {0.f, 0.f, 0.f, 0.f}, accFb = {0.f, 0.f, 0.f, 0.f};
#pragma unroll
    for (int t = 0; t < 5; ++t) {
      const u32x4 A = amask(xsF[t]);
      accFa = mfma(A, ring[(IW2 + 2 * t) % RING], accFa);
      accFb = mfma(A, ring[(IW2 + 1 + 2 * t) % RING], accFb);
    }
    LAS char* yb = L + L_Y + wave * YW;
    const u32 colb = (8 * c + 4 * (g & 1)) * 4;
    *(LAS f32x4*)(yb + bb * 640 + colb) = accFa;
    if (c < 4) *(LAS f32x4*)(yb + bb * 640 + 512 + colb) = accFb;
  }
  wg_barrier();
  VM0();
  if (wave != 0) {
    const int t = tid - 64;
    const int fb = t >= 80, tt = t - 80 * fb, q4 = tt / 20, u = tt - 20 * q4, half = q4 >> 1, rr = q4 & 1;
    if (t < 160 && u < ((fc & 1) ? 16 : 20)) {
      f32x2 s = {0.f, 0.f};
#pragma unroll
      for (int w = 0; w < 4; ++w) s += *(const LAS f32x2*)(L + L_Y + w * YW + fb * 640 + (8 * u + 4 * half + 2 * rr) * 4);
      const int ch = (fcol >> 3) + u, cw = ch >> 4, cc = ch & 15;
      const int rho = cw < 4 ? ((cw * 2 + half) * 4 + (cc >> 2)) * 2 + rr : 64 + (half * 2 + (cc >> 2)) * 2 + rr;
      GSTORE(gpair(s[0], s[1], tag), rsW, WS_YF + (72 * fb + rho) * YFS + (h * 2 + fa) * 64 + (cc & 3) * 16);
    }
  } else {
    u32 o_x, o_z, s_x, s_z;
    {
      u32 t0, t1, cnt = SPIN_CAP;
#define CHK1(O0, O1, O2, O3, S0, S1, S2, S3) \
      "v_accvgpr_read_b32 %[t0], " O1 "\n\tv_accvgpr_read_b32 %[t1], " O3 "\n\tv_accvgpr_read_b32 %[ox], " O0 "\n\tv_accvgpr_read_b32 %[oz], " O2 "\n\t" \
      "v_bitop3_b32 %[t0], %[t0], %[t1], %[thi] bitop3:0x7e\n\t" \
      "v_accvgpr_read_b32 %[t1], " S1 "\n\tv_accvgpr_read_b32 %[sx], " S0 "\n\tv_bitop3_b32 %[t0], %[t1], %[thi], %[t0] bitop3:0xbe\n\t" \
      "v_accvgpr_read_b32 %[t1], " S3 "\n\tv_accvgpr_read_b32 %[sz], " S2 "\n\tv_bitop3_b32 %[t0], %[t1], %[thi], %[t0] bitop3:0xbe\n\t" \
      "v_bitop3_b32 %[t1], %[ox], %[oz], %[tlo] bitop3:0x7e\n\tv_and_or_b32 %[t0], %[t1], %[ff], %[t0]\n\t" \
      "v_bitop3_b32 %[t1], %[sx], %[sz], %[tlo] bitop3:0x7e\n\tv_and_or_b32 %[t0], %[t1], %[ff], %[t0]\n\t" \
      "v_cmp_ne_u32 vcc, 0, %[t0]\n\t"
#define LD1(O, S) "buffer_load_dwordx4 " O ", %[oo], %[rs], 0 offen sc1\n\tbuffer_load_dwordx4 " S ", %[so], %[rs], 0 offen sc1\n\t"
      asm volatile(
        LD1("a[240:243]", "a[244:247]")
        "s_sleep %[sl]\n"
        "1:\n\t"
        LD1("a[248:251]", "a[252:255]")
        "s_waitcnt vmcnt(2)\n\t"
        CHK1("a240", "a241", "a242", "a243", "a244", "a245", "a246", "a247")
        "s_cbranch_vccz 2f\n\t"
        LD1("a[240:243]", "a[244:247]")
        "s_waitcnt vmcnt(2)\n\t"
        CHK1("a248", "a249", "a250", "a251", "a252", "a253", "a254", "a255")
        "s_cbranch_vccz 2f\n\t"
        "s_sub_u32 %[cnt], %[cnt], 1\n\t"
        "s_cmp_lg_u32 %[cnt], 0\n\t"
        "s_cbranch_scc1 1b\n"
        "2:"
        : [ox] "=&v"(o_x), [oz] "=&v"(o_z), [sx] "=&v"(s_x), [sz] "=&v"(s_z), [t0] "=&v"(t0), [t1] "=&v"(t1), [cnt] "+s"(cnt)
        : [oo] "v"(ooff), [so] "v"(osoff), [rs] "s"(rsW), [thi] "s"(tag.hi), [tlo] "s"(tag.lo), [sl] "n"(PSL1), [ff] "s"(0xFFu)
        : "vcc", "scc", "memory", "a240", "a241", "a242", "a243", "a244", "a245", "a246", "a247", "a248", "a249", "a250", "a251", "a252", "a253", "a254", "a255");
    }

    const float ms = gdec(s_x), ls = gdec(s_z);
    float M = fmaxf(ms, dpp<XOR1>(ms)); M = fmaxf(M, dpp<XOR2>(M));
    const float wv = __builtin_amdgcn_exp2f(ms - M);
    float Lt = ls * wv; Lt += dpp<XOR1>(Lt); Lt += dpp<XOR2>(Lt);
    const float scl = wv * __builtin_amdgcn_rcpf(Lt);
    float v0 = gdec(o_x) * scl, v1 = gdec(o_z) * scl;
    v0 += dpp<XOR1>(v0); v1 += dpp<XOR1>(v1); v0 += dpp<XOR2>(v0); v1 += dpp<XOR2>(v1);
    const float w0 = dpp<HMIR>(v0), w1_ = dpp<HMIR>(v1);
    const float o0 = cb4 ? w0 : v0, o1 = cb4 ? w1_ : v1, o2 = cb4 ? v0 : w0, o3 = cb4 ? v1 : w1_;
    *(LAS u32x4*)(L + L_A + lane * 16) = u32x4{pkbf(o0, o0), pkbf(o1, o1), pkbf(o2, o2), pkbf(o3, o3)};
  }
  SB();

  wg_barrier();
  {
    const u32x4 ov = *(const LAS u32x4*)(L + L_A + lane * 16);
    f32x4 accA = {0.f, 0.f, 0.f, 0.f}, accH = {0.f, 0.f, 0.f, 0.f};
#pragma unroll
    for (int t = 0; t < 4; ++t) {
      const u32x4 A = amask(ov[t]);
      accA = mfma(A, ring[(IWO + t) % RING], accA);
      accH = mfma(A, ring[(IWO + 4 + t) % RING], accH);
    }
    const u32 yoff = WS_YA + (72 * bb + ((wave * 2 + (g & 1)) * 4 + (c >> 2)) * 2) * YAS + (h * 8 + j8) * 64 + (c & 3) * 16;
    GSTORE(gpair(accA[0], accA[1], tag), rsW, yoff);
    GSTORE(gpair(accA[2], accA[3], tag), rsW, yoff + YAS);
    if (wave == 3 && c < 8) {
      const u32 yo2 = WS_YA + (72 * bb + 64 + ((g & 1) * 2 + (c >> 2)) * 2) * YAS + (h * 8 + j8) * 64 + (c & 3) * 16;
      GSTORE(gpair(accH[0], accH[1], tag), rsW, yo2);
      GSTORE(gpair(accH[2], accH[3], tag), rsW, yo2 + YAS);
    }
  }
  SB();

  if (reducer) {
    const u32 oa = WS_YA + rid * YAS + (64 * wave + (lane >> 2)) * 64 + (lane & 3) * 16;
    const u32 of = WS_YF + rid * YFS + (16 * wave + (lane >> 2)) * 64 + (lane & 3) * 16;
    u32 r0, r1, r2, r3, r4, r5, r6, r7, r8, r9;
    {
      u32 t0, t1, cnt = SPIN_CAP; u64 sv, d0, d1, d2, d3, d4;
#define LDP(D, DST, VO, OFF) "s_andn2_b64 exec, %[sv], " D "\n\tbuffer_load_dwordx4 " DST ", " VO ", %[rs], 0 offen" OFF " sc1\n\t"
#define LD2(A0, A1, A2, A3, F0) LDP("%[d0]", A0, "%[oa]", "") LDP("%[d1]", A1, "%[oa]", " offset:1024") LDP("%[d2]", A2, "%[oa]", " offset:2048") LDP("%[d3]", A3, "%[oa]", " offset:3072") LDP("%[d4]", F0, "%[of]", "")
#define RD2(D, X, Z, B0, B1, B2, B3) \
      "s_andn2_b64 exec, %[sv], " D "\n\t" \
      "v_accvgpr_read_b32 %[t0], " B1 "\n\tv_accvgpr_read_b32 %[t1], " B3 "\n\tv_accvgpr_read_b32 " X ", " B0 "\n\tv_accvgpr_read_b32 " Z ", " B2 "\n\t" \
      "v_bitop3_b32 %[t0], %[t0], %[t1], %[thi] bitop3:0x7e\n\tv_bitop3_b32 %[t1], " X ", " Z ", %[tlo] bitop3:0x7e\n\tv_and_or_b32 %[t0], %[t1], %[ff], %[t0]\n\t" \
      "v_cmp_eq_u32 vcc, 0, %[t0]\n\ts_or_b64 " D ", " D ", vcc\n\t"
#define END2 \
      "s_and_b64 vcc, %[d0], %[d1]\n\ts_and_b64 vcc, vcc, %[d2]\n\ts_and_b64 vcc, vcc, %[d3]\n\ts_and_b64 vcc, vcc, %[d4]\n\t" \
      "s_andn2_b64 vcc, %[sv], vcc\n\ts_cbranch_scc0 2f\n\t"
#define LDX LD2("a[160:163]", "a[164:167]", "a[168:171]", "a[172:175]", "a[176:179]")
#define LDY LD2("a[180:183]", "a[184:187]", "a[188:191]", "a[192:195]", "a[196:199]")
      asm volatile(
        "s_mov_b64 %[sv], exec\n\ts_mov_b64 %[d0], 0\n\ts_mov_b64 %[d1], 0\n\ts_mov_b64 %[d2], 0\n\ts_mov_b64 %[d3], 0\n\ts_mov_b64 %[d4], 0\n\t"
        "s_sleep %[s0]\n\t"
        LDX
        "s_sleep %[sl]\n"
        "1:\n\t"
        LDY
        "s_waitcnt vmcnt(5)\n\t"
        RD2("%[d0]", "%[r0]", "%[r1]", "a160", "a161", "a162", "a163") RD2("%[d1]", "%[r2]", "%[r3]", "a164", "a165", "a166", "a167")
        RD2("%[d2]", "%[r4]", "%[r5]", "a168", "a169", "a170", "a171") RD2("%[d3]", "%[r6]", "%[r7]", "a172", "a173", "a174", "a175")
        RD2("%[d4]", "%[r8]", "%[r9]", "a176", "a177", "a178", "a179")
        END2
        LDX
        "s_waitcnt vmcnt(5)\n\t"
        RD2("%[d0]", "%[r0]", "%[r1]", "a180", "a181", "a182", "a183") RD2("%[d1]", "%[r2]", "%[r3]", "a184", "a185", "a186", "a187")
        RD2("%[d2]", "%[r4]", "%[r5]", "a188", "a189", "a190", "a191") RD2("%[d3]", "%[r6]", "%[r7]", "a192", "a193", "a194", "a195")
        RD2("%[d4]", "%[r8]", "%[r9]", "a196", "a197", "a198", "a199")
        END2
        "s_sub_u32 %[cnt], %[cnt], 1\n\t"
        "s_cmp_lg_u32 %[cnt], 0\n\t"
        "s_cbranch_scc1 1b\n"
        "2:\n\t"
        "s_mov_b64 exec, %[sv]"
        : [r0] "=&v"(r0), [r1] "=&v"(r1), [r2] "=&v"(r2), [r3] "=&v"(r3), [r4] "=&v"(r4), [r5] "=&v"(r5), [r6] "=&v"(r6), [r7] "=&v"(r7), [r8] "=&v"(r8), [r9] "=&v"(r9),
          [t0] "=&v"(t0), [t1] "=&v"(t1), [cnt] "+s"(cnt), [sv] "=&s"(sv), [d0] "=&s"(d0), [d1] "=&s"(d1), [d2] "=&s"(d2), [d3] "=&s"(d3), [d4] "=&s"(d4)
        : [oa] "v"(oa), [of] "v"(of), [rs] "s"(rsW), [thi] "s"(tag.hi), [tlo] "s"(tag.lo), [sl] "n"(PSL2), [s0] "n"(PSL0), [ff] "s"(0xFFu)
        : "vcc", "scc", "memory",
          "a160", "a161", "a162", "a163", "a164", "a165", "a166", "a167", "a168", "a169", "a170", "a171", "a172", "a173", "a174", "a175", "a176", "a177", "a178", "a179",
          "a180", "a181", "a182", "a183", "a184", "a185", "a186", "a187", "a188", "a189", "a190", "a191", "a192", "a193", "a194", "a195", "a196", "a197", "a198", "a199");
    }
    float v[4] = {(gdec(r0) + gdec(r2)) + (gdec(r4) + gdec(r6)), (gdec(r1) + gdec(r3)) + (gdec(r5) + gdec(r7)), gdec(r8), gdec(r9)};
#pragma unroll
    for (int k = 0; k < 4; ++k) { v[k] += dpp<ROR4>(v[k]); v[k] += dpp<ROR8>(v[k]); v[k] = x32_sum(x16_sum(v[k])); }
    if (lane < 4) {
      *(LAS f32x2*)(L + L_FIN + (wave * 16 + 2 * lane) * 4) = f32x2{v[0], v[1]};
      *(LAS f32x2*)(L + L_FIN + (wave * 16 + 8 + 2 * lane) * 4) = f32x2{v[2], v[3]};
    }
    wg_barrier();

    if (tid < 8) {
      const LAS float* fp = (const LAS float*)(L + L_FIN);
      const float s = rbf((fp[tid] + fp[16 + tid]) + (fp[32 + tid] + fp[48 + tid]));
      const float y = rbf(rbf((fp[8 + tid] + fp[24 + tid]) + (fp[40 + tid] + fp[56 + tid])) + bflo(braw));
      const float gt = rbf(y * __builtin_amdgcn_rcpf(1.0f + __builtin_amdgcn_exp2f(y * -1.4426950408889634f)));
      ((GAS unsigned short*)out)[rb * DM + ocol] = __builtin_bit_cast(unsigned short, (__bf16)(bflo(xraw) + rbf(s * gt)));
    }
    if (bid == 0 && tid < 4) GSTORE((u32x4{tid == 0 ? (tag.hi << 8) | tag.lo : 0u, tid == 0 ? tag.hi >> 24 : 0u, 0u, 0u}), rsW, WS_E + tid * 16);
  }
}

extern "C" void kernel_launch(void* const* d_in, const int* in_sizes, int n_in, void* d_out, int out_size, void* d_ws, size_t ws_size, hipStream_t stream) {
  (void)hipFuncSetAttribute((const void*)fused_decode, hipFuncAttributeMaxDynamicSharedMemorySize, L_END);
  fused_decode<<<256, 256, L_END, stream>>>((const unsigned short*)d_in[0], (const char*)d_in[1], (const char*)d_in[2], (const char*)d_in[3], (const char*)d_in[4],
                                            (const char*)d_in[6], (const unsigned short*)d_in[7], (const char*)d_in[8], (const unsigned short*)d_in[9],
                                            (unsigned short*)d_out, (char*)d_ws);
}
```
